```python
import torch

def _softmax(x, axis=-1):
    return torch.softmax(x, dim=axis)
def _gelu(x):
    return torch.nn.functional.gelu(x, approximate="tanh")
def _silu(x):
    return torch.nn.functional.silu(x)
def _sigmoid(x):
    return torch.sigmoid(x)
def _layernorm(x, axis=-1, eps=1e-5):
    m = x.mean(dim=axis, keepdim=True)
    v = x.var(dim=axis, keepdim=True, unbiased=False)
    return (x - m) / torch.sqrt(v + eps)
def _scatter_add(tgt, idx, src, axis=0):
    return tgt.clone().index_add_(axis, idx, src)
def _segment_sum(src, seg, n):
    out = torch.zeros((n,) + src.shape[1:], dtype=src.dtype)
    return out.index_add_(0, seg, src)
def _pad(x, pw):
    flat = tuple(v for pair in reversed(pw) for v in pair)
    return torch.nn.functional.pad(x, flat)

kernel_name = 'gather_scatter__edge_featured_mpnn_egc__graph'

def setup_inputs():

    _w = (lambda a: (lambda t: t.to(torch.bfloat16) if t.is_floating_point() else t)(torch.as_tensor(a)))
    N, deg, D, De = 10000, 16, 256, max(16, 1)
    E = N * deg
    return {
        "x": _w((lambda *s: torch.randn(*s, dtype=torch.bfloat16) * 0.1)(N, D)),
        "e_attr": _w((lambda *s: torch.randn(*s, dtype=torch.bfloat16) * 0.1)(E, De)),
        "src": _w((lambda lo, hi, *s: torch.randint(lo, hi, s))(0, N, E).to(torch.int64)),
        "dst": _w((lambda lo, hi, *s: torch.randint(lo, hi, s))(0, N, E).to(torch.int64)),
        "u_w": _w((lambda *s: torch.randn(*s, dtype=torch.bfloat16) * 0.05)(D, D)),
        "v_w": _w((lambda *s: torch.randn(*s, dtype=torch.bfloat16) * 0.05)(D, D)),
        "g_w": _w((lambda *s: torch.randn(*s, dtype=torch.bfloat16) * 0.1)(2 * D + De, D)),
    }

def reference(x, e_attr, src, dst, u_w, v_w, g_w):
    N, D = x.shape
    g_in = torch.concatenate([x[dst], x[src], e_attr], axis=-1)
    eta = _sigmoid(g_in @ g_w)
    hv = x @ v_w
    msg = eta * hv[src]
    agg = _segment_sum(msg, dst, N)
    return _silu(x @ u_w + agg)
```

```cpp
#include <hip/hip_runtime.h>
#include <hip/hip_bf16.h>
#include <stdint.h>

static __device__ __forceinline__ float bf2f(uint16_t v) { return __uint_as_float(((uint32_t)v) << 16); }

__global__ void k_gemm(const uint16_t* x, const uint16_t* uw, const uint16_t* vw, const uint16_t* gw, float* T, int N) {
    int n = blockIdx.x;
    int c = threadIdx.x;
    __shared__ float xs[256];
    xs[c] = bf2f(x[n * 256 + c]);
    __syncthreads();
    float a = 0, b = 0, v = 0, u = 0;
    for (int k = 0; k < 256; k++) {
        float xv = xs[k];
        a += xv * bf2f(gw[k * 256 + c]);
        b += xv * bf2f(gw[(256 + k) * 256 + c]);
        v += xv * bf2f(vw[k * 256 + c]);
        u += xv * bf2f(uw[k * 256 + c]);
    }
    T[(size_t)n * 1024 + c] = a;
    T[(size_t)n * 1024 + 256 + c] = b;
    T[(size_t)n * 1024 + 512 + c] = v;
    T[(size_t)n * 1024 + 768 + c] = u;
}
__global__ void k_zero(float* agg, int n) { int i = blockIdx.x * 256 + threadIdx.x; if (i < n) agg[i] = 0.f; }
__global__ void k_edge(const uint16_t* ea, const int* src, const int* dst, const uint16_t* gw, const float* T, float* agg, int E) {
    int e = blockIdx.x;
    int c = threadIdx.x;
    int s = src[e], d = dst[e];
    float z = T[(size_t)d * 1024 + c] + T[(size_t)s * 1024 + 256 + c];
    for (int k = 0; k < 16; k++) z += bf2f(ea[e * 16 + k]) * bf2f(gw[(512 + k) * 256 + c]);
    float eta = 1.f / (1.f + __expf(-z));
    atomicAdd(&agg[(size_t)d * 256 + c], eta * T[(size_t)s * 1024 + 512 + c]);
}
__global__ void k_out(const float* T, const float* agg, __hip_bfloat16* out, int N) {
    int n = blockIdx.x, c = threadIdx.x;
    float v = T[(size_t)n * 1024 + 768 + c] + agg[(size_t)n * 256 + c];
    out[(size_t)n * 256 + c] = __float2bfloat16(v / (1.f + __expf(-v)));
}

extern "C" void kernel_launch(void* const* d_in, const int* in_sizes, int n_in,
                              void* d_out, int out_size, void* d_ws, size_t ws_size,
                              hipStream_t stream) {
    const uint16_t* x = (const uint16_t*)d_in[0];
    const uint16_t* ea = (const uint16_t*)d_in[1];
    const int* src = (const int*)d_in[2];
    const int* dst = (const int*)d_in[3];
    const uint16_t* uw = (const uint16_t*)d_in[4];
    const uint16_t* vw = (const uint16_t*)d_in[5];
    const uint16_t* gw = (const uint16_t*)d_in[6];
    int N = in_sizes[0] / 256;
    int E = in_sizes[2];
    float* T = (float*)d_ws;
    float* agg = T + (size_t)N * 1024;
    k_gemm<<<N, 256, 0, stream>>>(x, uw, vw, gw, T, N);
    k_zero<<<(N * 256 + 255) / 256, 256, 0, stream>>>(agg, N * 256);
    k_edge<<<E, 256, 0, stream>>>(ea, src, dst, gw, T, agg, E);
    k_out<<<N, 256, 0, stream>>>(T, agg, (__hip_bfloat16*)d_out, N);
}
```

```cpp
#include <hip/hip_runtime.h>
#include <hip/hip_bf16.h>
#include <stdint.h>
#include <type_traits>

typedef unsigned u32;
typedef unsigned long long u64;
typedef u32 u32x2 __attribute__((ext_vector_type(2)));
typedef u32 u32x4 __attribute__((ext_vector_type(4)));
typedef float f32x2 __attribute__((ext_vector_type(2)));
typedef float f32x4 __attribute__((ext_vector_type(4)));
typedef __bf16 bf16x2 __attribute__((ext_vector_type(2)));
typedef __bf16 bf16x8 __attribute__((ext_vector_type(8)));
typedef short s16x4 __attribute__((ext_vector_type(4)));
typedef __amdgpu_buffer_rsrc_t rsrc_t;
#define LAS __attribute__((address_space(3)))
#define DEV __device__ __forceinline__

constexpr int NN = 10000;
constexpr int NE = 160000;
constexpr int NR = 64;
constexpr int NW = 256;
constexpr int CHUNK = NE / NW;
constexpr int CAP = 32;
constexpr int LCAP = 2848;
constexpr int LDUM = LCAP + 64;
constexpr float NLOG2E = -1.4426950408889634f;
constexpr int SC1 = 16;
#define GAUX 16

constexpr u32 OFF_EPOCH = 0;
constexpr u32 OFF_FLAGA = 4096;
constexpr u32 OFF_FLAGB = OFF_FLAGA + 8 * 4096;
constexpr u32 OFF_T = OFF_FLAGB + 8 * 4096;
constexpr u32 SZ_T = 4u * NN * 256u;
constexpr u32 OFF_CNT = OFF_T + SZ_T + 4096;
constexpr u32 OFF_IDX = OFF_CNT + NR * NW * 4;
constexpr u32 OFF_AUX = OFF_IDX + NR * NW * CAP * 4 + 4096;
constexpr u32 AUX_ZERO = 0;
constexpr u32 AUX_EP = 1024;
constexpr u32 AUX_SZ = AUX_EP + (u32)NR * NW * CAP * 32;
constexpr u32 OOB = 0x7FFFFF00u;

constexpr int L_XS = 0;
constexpr int L_WS = 81920;
constexpr int PT_C = 157;
constexpr int L_PT = 0;
constexpr int L_META = (PT_C + 16) * 256;
constexpr int L_EREC = L_META + LDUM * 4;
static_assert(L_EREC + LCAP * 32 <= 147456, "edge-phase overlay");
constexpr int L_CTR = 147456;
constexpr int L_H1 = L_CTR + 64;
constexpr int L_H2 = L_H1 + 512;
constexpr int L_NST = L_H2 + 1024;
constexpr int L_BIN = L_NST + 1024;
constexpr int L_LUT = L_BIN + 256;
constexpr int L_SCR = L_LUT + 384;
constexpr int L_SRC = L_SCR + 256;
constexpr int L_TMP = L_SRC + LDUM * 2;
constexpr int L_END = L_TMP + 6400;
static_assert(L_END <= 163840 && LDUM * 2 <= 6400 && (L_EREC & 15) == 0 && (L_SRC & 15) == 0 && (L_TMP & 15) == 0, "LDS budget");

#define STAMP(i)
#define STAMPC(i)
#define STAMP_DECL
#define STAMP_FLUSH

DEV u32 fine_bin(u32 d) { return __umulhi(d * 64u, 6871948u); }
DEV u32 bin_start(u32 b) { return (b * 625u + 63u) >> 6; }
DEV u32 wswz(u32 k) { return ((k & 3) << 2) | ((k >> 2) & 3); }

DEV f32x4 mfma16(u32x4 a, u32x4 b, f32x4 c) {
    return __builtin_amdgcn_mfma_f32_16x16x32_bf16(__builtin_bit_cast(bf16x8, a), __builtin_bit_cast(bf16x8, b), c, 0, 0, 0);
}
DEV u32 cvtpk(float lo, float hi) {
    f32x2 v = {lo, hi};
    return __builtin_bit_cast(u32, __builtin_convertvector(v, bf16x2));
}
DEV float dot2(u32 w, u32 sel, float acc) {
    return __builtin_amdgcn_fdot2_f32_bf16(__builtin_bit_cast(bf16x2, w), __builtin_bit_cast(bf16x2, sel), acc, false);
}
DEV u32x2 trread(u32 lds_addr) {
    return __builtin_bit_cast(u32x2, __builtin_amdgcn_ds_read_tr16_b64_v4i16((LAS s16x4*)(uintptr_t)lds_addr));
}
DEV rsrc_t mkrsrc(const void* p, u32 bytes) { return __builtin_amdgcn_make_buffer_rsrc((void*)p, 0, bytes, 0x00020000); }
DEV u32 fresh_lane() {
    u32 l;
    asm volatile("v_mbcnt_lo_u32_b32 %0, -1, 0\n\tv_mbcnt_hi_u32_b32 %0, -1, %0" : "=v"(l));
    return l;
}
DEV u32x4 onehot8(int idx) {
    u32x4 r;
#pragma unroll
    for (int d = 0; d < 4; d++) r[d] = idx == 2 * d ? 0x00003F80u : (idx == 2 * d + 1 ? 0x3F800000u : 0u);
    return r;
}
DEV void st_agent(u32* p, u32 v) { __hip_atomic_store(p, v, __ATOMIC_RELAXED, __HIP_MEMORY_SCOPE_AGENT); }
DEV void barrier() {
    asm volatile("s_waitcnt lgkmcnt(0)\n\ts_barrier" ::: "memory");
}
DEV void wait_vm0() { __builtin_amdgcn_s_waitcnt(0x0F70); asm volatile("" ::: "memory"); }
DEV void group_barrier(LAS char* lds, int ctr, u32 n, u32 lane) {
    const u32 a = (u32)(uintptr_t)lds + L_CTR + 4 * ctr;
    u32 v, one = 1;
    asm volatile("s_waitcnt lgkmcnt(0)" ::: "memory");
    if (lane == 0) asm volatile("ds_add_u32 %0, %1" :: "v"(a), "v"(one) : "memory");
    for (int it = 0; it < (1 << 22); it++) {
        asm volatile("ds_read_b32 %0, %1\n\ts_waitcnt lgkmcnt(0)" : "=&v"(v) : "v"(a) : "memory");
        if (__builtin_amdgcn_readfirstlane(v) >= n) break;
        asm volatile("s_sleep 1" ::: "memory");
    }
}
DEV u32 mad16lo(u32 a, u32 b, u32 c) { u32 r; asm("v_mad_u32_u16 %0, %1, %2, %3" : "=v"(r) : "v"(a), "v"(b), "v"(c)); return r; }
DEV u32 mad16hi(u32 a, u32 b, u32 c) { u32 r; asm("v_mad_u32_u16 %0, %1, %2, %3 op_sel:[1,0,0,0]" : "=v"(r) : "v"(a), "v"(b), "v"(c)); return r; }
DEV void poll_flags(const char* ws, u32 off, u32 replica, u32 epoch, u32 lane) {
    const char* p = ws + off + replica * 4096 + lane * 16;
    for (int it = 0; it < (1 << 20); it++) {
        u32x4 v;
        asm volatile("global_load_dwordx4 %0, %1, off sc1\n\ts_waitcnt vmcnt(0)" : "=&v"(v) : "v"(p) : "memory");
        const bool ok = v.x == epoch && v.y == epoch && v.z == epoch && v.w == epoch;
        if (__all(ok)) break;
        asm volatile("s_sleep 2" ::: "memory");
    }
}

struct Tile { u32x4 w[4]; u32x4 a; };

struct EdgeLoop {
    rsrc_t rT;
    const LAS uint16_t* lsrc; const LAS uint16_t* lmeta;
    u32 c16, r256, abase, sel;
    u32x4 Bz[4], Bid;

    DEV void issue(Tile& T, int pos) const {
        const u32x2 o = *(const LAS u32x2*)(lsrc + pos);
        const u32 m = lmeta[2 * pos];
        T.w[0] = __builtin_amdgcn_raw_buffer_load_b128(rT, mad16lo(o.x, r256, c16), 0, SC1);
        T.w[1] = __builtin_amdgcn_raw_buffer_load_b128(rT, mad16hi(o.x, r256, c16), 0, SC1);
        T.w[2] = __builtin_amdgcn_raw_buffer_load_b128(rT, mad16lo(o.y, r256, c16), 0, SC1);
        T.w[3] = __builtin_amdgcn_raw_buffer_load_b128(rT, mad16hi(o.y, r256, c16), 0, SC1);
        T.a = *(const LAS u32x4*)(uintptr_t)((m << 4) + abase);
    }
    DEV void compute(const Tile& T, u32x2 (&pk)[4], u32x2& S) const {
        f32x4 Z[4];
#pragma unroll
        for (int t = 0; t < 4; t++) Z[t] = mfma16(T.a, Bz[t], f32x4{0, 0, 0, 0});
        const f32x4 OH = mfma16(T.a, Bid, f32x4{0, 0, 0, 0});
        S.x = __builtin_amdgcn_perm(__float_as_uint(OH[1]), __float_as_uint(OH[0]), 0x07060302u);
        S.y = __builtin_amdgcn_perm(__float_as_uint(OH[3]), __float_as_uint(OH[2]), 0x07060302u);
        float msg[4][4];
#pragma unroll
        for (int i = 0; i < 4; i++) {
#pragma unroll
            for (int tp = 0; tp < 4; tp += 2) {
                const float z0 = dot2(T.w[i][tp], sel, Z[tp][i]), z1 = dot2(T.w[i][tp + 1], sel, Z[tp + 1][i]);
                f32x2 p = {__builtin_amdgcn_exp2f(z0), __builtin_amdgcn_exp2f(z1)};
                p = p + 1.0f;
                const f32x2 r = {__builtin_amdgcn_rcpf(p.x), __builtin_amdgcn_rcpf(p.y)};
                const f32x2 h = {__uint_as_float(T.w[i][tp]), __uint_as_float(T.w[i][tp + 1])};
                const f32x2 m = r * h;
                msg[i][tp] = m.x; msg[i][tp + 1] = m.y;
            }
        }
#pragma unroll
        for (int t = 0; t < 4; t++) { pk[t].x = cvtpk(msg[0][t], msg[1][t]); pk[t].y = cvtpk(msg[2][t], msg[3][t]); }
    }
    DEV void run_simple(f32x4 (&agg)[4], int pos, int nt) const {
        for (int t = 0; t < nt; t++) {
            Tile T0;
            u32x2 pk0[4], S0;
            issue(T0, pos + 16 * t);
            compute(T0, pk0, S0);
#pragma unroll
            for (int k = 0; k < 4; k++)
                agg[k] = mfma16(u32x4{S0.x, S0.y, 0u, 0u}, u32x4{pk0[k].x, pk0[k].y, 0u, 0u}, agg[k]);
        }
    }
    DEV void run(f32x4 (&agg)[4], int pos, int nt) const {
        if (nt <= 0) return;
        Tile T0, T1;
        u32x2 pk0[4], pk1[4], S0, S1;
        issue(T0, pos);
        int t = 0;
        for (; t + 2 < nt; t += 2) {
            issue(T1, pos + 16 * (t + 1));
            __builtin_amdgcn_sched_barrier(0);
            compute(T0, pk0, S0);
            __builtin_amdgcn_sched_barrier(0);
            issue(T0, pos + 16 * (t + 2));
            __builtin_amdgcn_sched_barrier(0);
            compute(T1, pk1, S1);
#pragma unroll
            for (int k = 0; k < 4; k++)
                agg[k] = mfma16(u32x4{S0.x, S0.y, S1.x, S1.y}, u32x4{pk0[k].x, pk0[k].y, pk1[k].x, pk1[k].y}, agg[k]);
            __builtin_amdgcn_sched_barrier(0);
        }
        if (t + 1 < nt) {
            issue(T1, pos + 16 * (t + 1));
            __builtin_amdgcn_sched_barrier(0);
            compute(T0, pk0, S0);
            __builtin_amdgcn_sched_barrier(0);
            compute(T1, pk1, S1);
#pragma unroll
            for (int k = 0; k < 4; k++)
                agg[k] = mfma16(u32x4{S0.x, S0.y, S1.x, S1.y}, u32x4{pk0[k].x, pk0[k].y, pk1[k].x, pk1[k].y}, agg[k]);
        } else {
            compute(T0, pk0, S0);
#pragma unroll
            for (int k = 0; k < 4; k++)
                agg[k] = mfma16(u32x4{S0.x, S0.y, 0u, 0u}, u32x4{pk0[k].x, pk0[k].y, 0u, 0u}, agg[k]);
        }
    }
};

template <int NK, typename RecT>
DEV int sort_batch(LAS char* lds, const u32 (&word)[NK], const u32 (&rec)[NK], u32 validmask, u32 nullrec, u32 lane, int wv, u32 rstart, u32 r) {
    LAS u32* hist = (LAS u32*)(lds + L_H2);
    LAS u32* nst = (LAS u32*)(lds + L_NST);
    LAS u32* bin = (LAS u32*)(lds + L_BIN);
    LAS uint16_t* lsrc = (LAS uint16_t*)(lds + L_SRC);
    LAS u32* lmeta = (LAS u32*)(lds + L_META);
    LAS RecT* lrec = (LAS RecT*)(lds + L_TMP);
    const u32 lb = (u32)(uintptr_t)lds;
    u32 rank[NK], key[NK];
#pragma unroll
    for (int k = 0; k < NK; k++) key[k] = (validmask & (1u << k)) ? (word[k] & 0xff) : 192u + lane;
#pragma unroll
    for (int k = 0; k < NK; k++) rank[k] = __hip_atomic_fetch_add(&hist[key[k]], 1u, __ATOMIC_RELAXED, __HIP_MEMORY_SCOPE_WORKGROUP);
    barrier();
    const u32 j = lane & 15;
    const u32 n0 = bin_start(r * 16 + j) - rstart, nb = bin_start(r * 16 + j + 1) - rstart - n0;
    u32 h[10], tot = 0;
#pragma unroll
    for (int k = 0; k < 10; k++) h[k] = hist[n0 + k];
    h[9] = nb > 9 ? h[9] : 0;
#pragma unroll
    for (int k = 0; k < 10; k++) tot += h[k];
    const u32 padded = (tot + 15) & ~15u;
    u32 inc = padded;
#pragma unroll
    for (int d = 1; d < 16; d <<= 1) { const u32 o = __shfl_up(inc, d, 16); if (j >= (u32)d) inc += o; }
    const u32 start = inc - padded;
    const u32 total = __builtin_amdgcn_readlane(inc, 15);
    if (total > (u32)LCAP || bin[33]) return -1;
    if (lane < 16) {
        u32 s = start;
#pragma unroll
        for (int k = 0; k < 10; k++) { if (k < 9 || nb > 9) nst[n0 + k] = s | (k << 16); s += h[k]; }
        bin[j] = start; bin[16 + j] = tot;
    }
    u32 nn[NK];
#pragma unroll
    for (int k = 0; k < NK; k++) nn[k] = nst[key[k]];
    const u32 erec16 = (lb + L_EREC) >> 4, lut16 = (lb + L_LUT) >> 4;
#pragma unroll
    for (int k = 0; k < NK; k++) {
        const u32 pos = (validmask & (1u << k)) ? (nn[k] & 0xffff) + rank[k] : LCAP + lane;
        lsrc[pos] = (uint16_t)(word[k] >> 8);
        lmeta[pos] = (erec16 + 2 * pos) | ((lut16 + (nn[k] >> 16)) << 16);
        if (sizeof(RecT) == 2 || pos < 1600) lrec[pos] = (RecT)rec[k];
    }
    u32 rk = 0;
#pragma unroll
    for (int jj = 0; jj < 16; jj++) { const u32 o = __builtin_amdgcn_readlane(padded, jj); rk += (o > padded || (o == padded && (u32)jj < j)) ? 1u : 0u; }
    const u32 slot4 = wv >> 2, want = 4 * slot4 + ((slot4 & 1) ? 3 - (wv & 3) : (wv & 3));
    const int mybin = __builtin_ctz((u32)__builtin_amdgcn_ballot_w64(rk == want) & 0xffffu);
    {
        const u32 s = __builtin_amdgcn_readlane(start, mybin), l = __builtin_amdgcn_readlane(tot, mybin);
        const u32 p = s + l + lane;
        if (lane < 16 && l + lane < ((l + 15) & ~15u)) { lsrc[p] = 0; lmeta[p] = (erec16 + 2 * p) | ((lut16 + 15) << 16); lrec[p] = (RecT)nullrec; }
    }
    if (wv == 0 && lane == 0) bin[32] = total;
    barrier();
    return mybin;
}
template <typename RecT>
DEV void fetch_records(LAS char* lds, const char* base, const char* zerorec, u32 nullrec, u32 lane, int wv) {
    const LAS RecT* lrec = (const LAS RecT*)(lds + L_TMP);
    const int total = __builtin_amdgcn_readfirstlane(((const LAS u32*)(lds + L_BIN))[32]);
    for (int p = wv; p * 32 < total; p += 16) {
        const u32 pos = p * 32 + (lane >> 1);
        const u32 id = (int)pos < total ? (u32)lrec[pos] : nullrec;
        const char* a = (id == nullrec ? zerorec : base + (size_t)id * 32) + (lane & 1) * 16;
        __builtin_amdgcn_global_load_lds((const u32*)a, (LAS u32*)(lds + L_EREC + p * 1024), 16, 0, SC1);
    }
}

__global__ void __launch_bounds__(1024, 4)
mpnn_fused(const uint16_t* __restrict__ x, const uint16_t* __restrict__ ea, const int* __restrict__ src, const int* __restrict__ dst,
           const uint16_t* __restrict__ uw, const uint16_t* __restrict__ vw, const uint16_t* __restrict__ gw,
           char* __restrict__ ws, uint16_t* __restrict__ out) {
    extern __shared__ __attribute__((aligned(16))) char smem[];
    LAS char* lds = (LAS char*)smem;
    const u32 lbase = (u32)(uintptr_t)lds;
    const int tid = threadIdx.x;
    const u32 lane = tid & 63;
    const int wv = __builtin_amdgcn_readfirstlane(tid >> 6);
    const u32 b = blockIdx.x;
    const u32 g = b & 3, r = ((b >> 3) << 1) + ((b >> 2) & 1);
    const u32 c = lane & 15, q = lane >> 4;
    const u32 rstart = bin_start(r * 16), rsize = bin_start(r * 16 + 16) - rstart;
    STAMP_DECL;
    STAMP(0);

    const bool g1 = wv < 8;
    const u32 t2 = tid - 512;
    u32 ed0 = 0, es0 = 0, ed1 = 0, es1 = 0;
    const bool v1 = t2 < (u32)(CHUNK - 512);
    if (g1) {
#pragma unroll
        for (int i = 0; i < 10; i++) {
            const u32 p = wv + 8 * i, row = 2 * p + (lane >> 5), ch = (lane & 31) ^ (row & 15);
            const u32 grow = min(rstart + row, (u32)NN - 1);
            __builtin_amdgcn_global_load_lds((const u32*)(x + (size_t)grow * 256 + ch * 8), (LAS u32*)(lds + L_XS + p * 1024), 16, 0, 0);
        }
#pragma unroll
        for (int i = 0; i < 8; i++) {
            const u32 p = wv + 8 * i, k = 4 * p + (lane >> 4), ch = (lane & 15) ^ wswz(k);
            const uint16_t* s = ch < 8 ? gw + (256 + k) * 256 + 64 * g + 8 * ch : vw + k * 256 + 64 * g + 8 * (ch - 8);
            __builtin_amdgcn_global_load_lds((const u32*)s, (LAS u32*)(lds + L_WS + p * 1024), 16, 0, 0);
        }
    } else {
        const u32 e0 = b * CHUNK + t2, e1 = v1 ? e0 + 512 : e0;
        ed0 = dst[e0]; es0 = src[e0]; ed1 = dst[e1]; es1 = src[e1];
        if (wv < 11) {
            const u32 ln = min((wv - 8) * 64 + lane, 156u);
            __builtin_amdgcn_global_load_lds((const u32*)(ea + (size_t)b * CHUNK * 16 + ln * 64), (LAS u32*)(lds + L_SCR), 4, 0, 0);
        }
    }
    const u32 epoch_in = __hip_atomic_load((const u32*)(ws + OFF_EPOCH), __ATOMIC_RELAXED, __HIP_MEMORY_SCOPE_AGENT);
    if (tid < 16) ((LAS u32*)(lds + L_CTR))[tid] = 0;
    if (tid >= 64 && tid < 192) ((LAS u32*)(lds + L_H1))[tid - 64] = 0;
    if (tid >= 256 && tid < 512) ((LAS u32*)(lds + L_H2))[tid - 256] = 0;
    if (tid == 0) ((LAS u32*)(lds + L_BIN))[33] = 0;
    if (tid >= 512 && tid < 512 + 96) {
        const int i = tid - 512, row = i >> 2, d = i & 3;
        ((LAS u32*)(lds + L_LUT))[i] = row - 8 == 2 * d ? 0x00003F80u : (row - 8 == 2 * d + 1 ? 0x3F800000u : 0u);
    }
    barrier();
    wait_vm0();
    const u32 epoch = __builtin_amdgcn_readfirstlane(epoch_in) + 1;
    STAMP(1);

    const int mw = (wv >> 2) & 1, tau = wv & 3;
    auto gemm = [&](f32x4 (&acc)[5][2], auto tr_c) {
        constexpr bool TR = decltype(tr_c)::value;
#pragma unroll
        for (int m = 0; m < 5; m++) { acc[m][0] = f32x4{0, 0, 0, 0}; acc[m][1] = f32x4{0, 0, 0, 0}; }
#pragma unroll
        for (int s = 0; s < 8; s++) {
            u32x4 B[2], A[5];
#pragma unroll
            for (int h = 0; h < 2; h++) {
#pragma unroll
                for (int t = 0; t < 2; t++) {
                    const u32 k = 32 * s + 8 * q + 4 * t + (c >> 2);
                    const u32 ch = 2 * (tau + 4 * h) + ((lane & 3) >> 1);
                    const u32x2 v = trread(lbase + L_WS + 256 * k + 16 * (ch ^ wswz(k)) + 8 * (lane & 1));
                    B[h][2 * t] = v.x; B[h][2 * t + 1] = v.y;
                }
            }
#pragma unroll
            for (int m = 0; m < 5; m++) A[m] = *(const LAS u32x4*)(lds + L_XS + (80 * mw + 16 * m + c) * 512 + 16 * ((4 * s + q) ^ c));
#pragma unroll
            for (int m = 0; m < 5; m++) {
                acc[m][0] = TR ? mfma16(B[0], A[m], acc[m][0]) : mfma16(A[m], B[0], acc[m][0]);
                acc[m][1] = TR ? mfma16(B[1], A[m], acc[m][1]) : mfma16(A[m], B[1], acc[m][1]);
            }
        }
    };

    f32x4 accP[5][2];
    if (g1) {
        group_barrier(lds, 0, 8, lane);
        STAMP(2);
        f32x4 acc[5][2];
        gemm(acc, std::true_type{});
        STAMP(3);
        const rsrc_t rT = mkrsrc(ws + OFF_T + ((size_t)g * NN + rstart) * 256, rsize * 256);
#pragma unroll
        for (int m = 0; m < 5; m++) {
            u32x4 w;
#pragma unroll
            for (int i = 0; i < 4; i++) {
                const u32 lo = cvtpk(acc[m][0][i] * NLOG2E, 0.f) & 0xffffu;
                const u32 u = __float_as_uint(acc[m][1][i]);
                const int mag = (int)(u & 0x7fffffffu) + 0x8000 - (int)lo;
                w[i] = (u & 0x80000000u) | ((u32)max(mag, 0) & 0x7fff0000u) | lo;
            }
            __builtin_amdgcn_raw_buffer_store_b128(w, rT, (80 * mw + 16 * m + c) * 256 + (16 * tau + 4 * q) * 4, 0, SC1);
        }
        STAMP(4);
        barrier();
        barrier();
        STAMP(5);
        gemm(accP, std::false_type{});
        STAMP(6);
    } else {
        LAS u32* h1 = (LAS u32*)(lds + L_H1);
        LAS u32* tslot = (LAS u32*)(lds + L_TMP);
        LAS u32* tword = tslot + 640;
        LAS uint16_t* tperm = (LAS uint16_t*)(tword + 640);
        const u32 r0 = fine_bin(ed0) >> 4, r1 = fine_bin(ed1) >> 4;
        const u32 k0 = __hip_atomic_fetch_add(h1 + r0, 1u, __ATOMIC_RELAXED, __HIP_MEMORY_SCOPE_WORKGROUP);
        const u32 k1 = __hip_atomic_fetch_add(h1 + (v1 ? r1 : 64 + lane), 1u, __ATOMIC_RELAXED, __HIP_MEMORY_SCOPE_WORKGROUP);
        group_barrier(lds, 1, 8, lane);
        const u32 hc = h1[lane];
        u32 inc = hc;
#pragma unroll
        for (int d = 1; d < 64; d <<= 1) { const u32 o = __shfl_up(inc, d, 64); if (lane >= (u32)d) inc += o; }
        const u32 st = inc - hc;
        const u32 p0 = __shfl(st, r0, 64) + k0, p1 = __shfl(st, r1, 64) + k1;
        tslot[p0] = k0 < CAP ? (r0 * NW + b) * CAP + k0 : OOB;
        tword[p0] = (es0 << 8) | (ed0 - bin_start(r0 * 16));
        tperm[p0] = (uint16_t)t2;
        if (v1) {
            tslot[p1] = k1 < CAP ? (r1 * NW + b) * CAP + k1 : OOB;
            tword[p1] = (es1 << 8) | (ed1 - bin_start(r1 * 16));
            tperm[p1] = (uint16_t)(t2 + 512);
        }
        group_barrier(lds, 2, 8, lane);
        STAMP(2);
        const rsrc_t rE = mkrsrc(ea + (size_t)b * CHUNK * 16, CHUNK * 32);
        const rsrc_t rI = mkrsrc(ws + OFF_IDX, NR * NW * CAP * 4);
        const rsrc_t rp = mkrsrc(ws + OFF_AUX, AUX_SZ);
        u32x4 ev[3]; u32 eo[3];
#pragma unroll
        for (int i = 0; i < 3; i++) {
            const u32 jj = min(t2 + 512 * i, 2u * CHUNK - 1), pos = jj >> 1;
            const u32 sl = tslot[pos];
            ev[i] = __builtin_amdgcn_raw_buffer_load_b128(rE, (u32)tperm[pos] * 32 + (jj & 1) * 16, 0, 0);
            eo[i] = (sl == OOB || t2 + 512 * i >= 2u * CHUNK) ? OOB : AUX_EP + sl * 32 + (jj & 1) * 16;
        }
        u32x4 wp[8];
#pragma unroll
        for (int i = 0; i < 8; i++) {
            const u32 p = (wv - 8) + 8 * i, k = 4 * p + (lane >> 4), ch = (lane & 15) ^ wswz(k);
            const uint16_t* s = ch < 8 ? gw + k * 256 + 64 * g + 8 * ch : uw + k * 256 + 64 * g + 8 * (ch - 8);
            wp[i] = *(const u32x4*)s;
        }
#pragma unroll
        for (int i = 0; i < 2; i++) {
            const u32 pos = min(t2 + 512 * i, (u32)CHUNK - 1);
            const u32 sl = tslot[pos];
            __builtin_amdgcn_raw_buffer_store_b32(tword[pos], rI, (sl == OOB || t2 + 512 * i >= (u32)CHUNK) ? OOB : sl * 4, 0, SC1);
        }
#pragma unroll
        for (int i = 0; i < 3; i++) __builtin_amdgcn_raw_buffer_store_b128(ev[i], rp, eo[i], 0, SC1);
        if (t2 < NR) st_agent((u32*)(ws + OFF_CNT) + t2 * NW + b, h1[t2]);
        if (b == 0 && t2 >= 64 && t2 < 72) st_agent((u32*)(ws + OFF_AUX + AUX_ZERO) + (t2 - 64), 0u);
        STAMP(3);
        asm volatile("s_waitcnt vmcnt(0)" ::: "memory");
        group_barrier(lds, 3, 8, lane);
        if (t2 < 8) st_agent((u32*)(ws + OFF_FLAGA + t2 * 4096) + b, epoch);
        STAMP(4);
        barrier();
#pragma unroll
        for (int i = 0; i < 8; i++) *(LAS u32x4*)(lds + L_WS + ((wv - 8) + 8 * i) * 1024 + lane * 16) = wp[i];
        barrier();
        STAMP(5);
        if (wv == 8) poll_flags(ws, OFF_FLAGA, b & 7, epoch, lane);
        STAMP(6);
    }
    barrier();
    STAMP(7);

    const u32 ln2 = fresh_lane(), td2 = wv * 64 + ln2;
    const u32 cell = r * NW + (td2 >> 2), k0 = (td2 & 3) * 8;
    const rsrc_t rC = mkrsrc(ws + OFF_CNT, NR * NW * 4 + NR * NW * CAP * 4);
    const u32 cnt = __builtin_amdgcn_raw_buffer_load_b32(rC, cell * 4, 0, SC1);
    const u32x4 i0 = __builtin_amdgcn_raw_buffer_load_b128(rC, NR * NW * 4 + (cell * CAP + k0) * 4, 0, SC1);
    const u32x4 i1 = __builtin_amdgcn_raw_buffer_load_b128(rC, NR * NW * 4 + (cell * CAP + k0 + 4) * 4, 0, SC1);
    const u32 cwv = gw[(512 + wv) * 256 + 64 * g + ln2];
    asm volatile("" ::: "memory");
    if (g1) {
#pragma unroll
        for (int m = 0; m < 5; m++) {
#pragma unroll
            for (int i = 0; i < 4; i++) {
                const u32 row = 80 * mw + 16 * m + 4 * q + i;
                const u32 pa = cvtpk(accP[m][0][i] * NLOG2E, accP[m][1][i]);
                const u32 pos = 16 * (c & 3) + 4 * tau + (c >> 2);
                if (row < (u32)PT_C) {
                    *(LAS uint16_t*)(lds + L_PT + row * 256 + pos * 2) = (uint16_t)pa;
                    *(LAS uint16_t*)(lds + L_PT + row * 256 + 128 + pos * 2) = (uint16_t)(pa >> 16);
                }
            }
        }
    }
    asm volatile("s_waitcnt vmcnt(4)" ::: "memory");
    barrier();
    STAMP(8);
    if (td2 < 8) st_agent((u32*)(ws + OFF_FLAGB + td2 * 4096) + b, epoch);
    *(LAS uint16_t*)(lds + L_PT + (PT_C + wv) * 256 + (16 * (ln2 & 3) + (ln2 >> 2)) * 2) = (uint16_t)cvtpk(__uint_as_float(cwv << 16) * NLOG2E, 0.f);

    LAS u32* bin = (LAS u32*)(lds + L_BIN);
    if (cnt > (u32)CAP) bin[33] = 1;
    const char* zerorec = ws + OFF_AUX + AUX_ZERO;
    int mybin;
    {
        u32 word[8] = {i0.x, i0.y, i0.z, i0.w, i1.x, i1.y, i1.z, i1.w}, rec[8];
        u32 vm = 0;
#pragma unroll
        for (int k = 0; k < 8; k++) { rec[k] = (td2 >> 2) * CAP + k0 + k; if (k0 + k < min(cnt, (u32)CAP)) vm |= 1u << k; }
        STAMP(9);
        mybin = sort_batch<8, uint16_t>(lds, word, rec, vm, 0xFFFFu, lane, wv, rstart, r);
    }
    const bool fast = mybin >= 0;
    if (fast) fetch_records<uint16_t>(lds, ws + OFF_AUX + AUX_EP + (size_t)r * NW * CAP * 32, zerorec, 0xFFFFu, lane, wv);
    STAMP(10);

    const int obin = fast ? mybin : wv;
    const u32 nstart = bin_start(r * 16 + obin), bsize = bin_start(r * 16 + obin + 1) - nstart;
    EdgeLoop L;
    L.rT = mkrsrc(ws + OFF_T + (size_t)g * NN * 256, NN * 256);
    L.lsrc = (const LAS uint16_t*)(lds + L_SRC) + 4 * q; L.lmeta = (const LAS uint16_t*)(lds + L_META) + 2 * c + (q < 2 ? 0 : 1);
    L.c16 = c * 16;
    L.sel = 0x00003F80u;
    L.r256 = 256;
    asm volatile("" : "+v"(L.sel), "+v"(L.r256));
    L.abase = q < 2 ? 16 * q : 16 * (8 - 8 * (q - 2));
    f32x4 agg[4];
    {
        const u32 rr = 8 * (q & 1) + (c >> 2), row0 = nstart - rstart;
        const u32 col = 8 * (lane & 3);
        const u32 pz0 = lbase + L_PT + (q < 2 ? PT_C + rr : row0 + rr) * 256 + col;
        const u32 pz1 = pz0 + 4 * 256;
        const u32 pu0 = lbase + L_PT + min(row0 + rr, rsize - 1) * 256 + 128 + col;
        const u32 pu1 = lbase + L_PT + min(row0 + rr + 4, rsize - 1) * 256 + 128 + col;
        const u32x4 aid = onehot8((int)c - 8 * (int)q);
        L.Bid = onehot8(q < 2 ? -1 : (int)c - 8 * ((int)q - 2));
#pragma unroll
        for (int t = 0; t < 4; t++) {
            const u32x2 z0 = trread(pz0 + 32 * t), z1 = trread(pz1 + 32 * t), u0 = trread(pu0 + 32 * t), u1 = trread(pu1 + 32 * t);
            L.Bz[t] = u32x4{z0.x, z0.y, z1.x, z1.y};
            agg[t] = mfma16(aid, u32x4{u0.x, u0.y, u1.x, u1.y}, f32x4{0, 0, 0, 0});
        }
    }
    if (wv == 0) poll_flags(ws, OFF_FLAGB, b & 7, epoch, lane);
    wait_vm0();
    barrier();
    STAMP(11);
    STAMPC(14);
    if (b == 0 && tid == 0) st_agent((u32*)(ws + OFF_EPOCH), epoch);

    if (fast) {
        const int pos = __builtin_amdgcn_readfirstlane(bin[mybin]), len = __builtin_amdgcn_readfirstlane(bin[16 + mybin]);
        L.run(agg, pos, (len + 15) >> 4);
    } else {
        const u32 ln = fresh_lane();
        const int td = wv * 64 + ln;
        LAS u32* hist = (LAS u32*)(lds + L_H2);
        for (int e0 = 0; e0 < NE; e0 += 1024) {
            barrier();
            if (td < 256) hist[td] = 0;
            if (td == 0) bin[33] = 0;
            barrier();
            u32 word[1] = {0}, rec[1] = {0}, vm = 0;
            const int ee = e0 + td;
            if (ee < NE) {
                const u32 d = dst[ee];
                if ((fine_bin(d) >> 4) == r) { word[0] = ((u32)src[ee] << 8) | (d - rstart); rec[0] = ee; vm = 1; }
            }
            sort_batch<1, u32>(lds, word, rec, vm, 0xFFFFFFFFu, ln, wv, rstart, r);
            fetch_records<u32>(lds, (const char*)ea, zerorec, 0xFFFFFFFFu, ln, wv);
            wait_vm0();
            barrier();
            const int pos = __builtin_amdgcn_readfirstlane(bin[wv]), len = __builtin_amdgcn_readfirstlane(bin[16 + wv]);
            L.run_simple(agg, pos, (len + 15) >> 4);
        }
    }
    STAMP(12);
    STAMPC(15);

    const u32 le = fresh_lane(), ce = le & 15, qe = le >> 4;
#pragma unroll
    for (int i = 0; i < 4; i++) {
        float y[4];
#pragma unroll
        for (int t = 0; t < 4; t++) {
            const float v = agg[t][i];
            y[t] = v * __builtin_amdgcn_rcpf(1.0f + __builtin_amdgcn_exp2f(v * NLOG2E));
        }
        const u32 slot = 4 * qe + i;
        if (slot < bsize) *(u32x2*)(out + (size_t)(nstart + slot) * 256 + 64 * g + 4 * ce) = u32x2{cvtpk(y[0], y[1]), cvtpk(y[2], y[3])};
    }
    STAMP(13);
    STAMP_FLUSH;
}

extern "C" void kernel_launch(void* const* d_in, const int* in_sizes, int n_in,
                              void* d_out, int out_size, void* d_ws, size_t ws_size,
                              hipStream_t stream) {
    const uint16_t* x = (const uint16_t*)d_in[0];
    const uint16_t* ea = (const uint16_t*)d_in[1];
    const int* src = (const int*)d_in[2];
    const int* dst = (const int*)d_in[3];
    const uint16_t* uw = (const uint16_t*)d_in[4];
    const uint16_t* vw = (const uint16_t*)d_in[5];
    const uint16_t* gw = (const uint16_t*)d_in[6];
    (void)hipFuncSetAttribute((const void*)mpnn_fused, hipFuncAttributeMaxDynamicSharedMemorySize, L_END);
    mpnn_fused<<<256, 1024, L_END, stream>>>(x, ea, src, dst, uw, vw, gw, (char*)d_ws, (uint16_t*)d_out);
}
```

```cpp
#define LEANDMA
#define FBLDS
#define G2PRIO
#define CODEPF 27648
#define PRIO
#define BURST1
#define REP 1
#include <hip/hip_runtime.h>
#include <hip/hip_bf16.h>
#include <stdint.h>
#include <type_traits>

typedef unsigned u32;
typedef unsigned long long u64;
typedef u32 u32x2 __attribute__((ext_vector_type(2)));
typedef u32 u32x4 __attribute__((ext_vector_type(4)));
typedef float f32x2 __attribute__((ext_vector_type(2)));
typedef float f32x4 __attribute__((ext_vector_type(4)));
typedef __bf16 bf16x2 __attribute__((ext_vector_type(2)));
typedef __bf16 bf16x8 __attribute__((ext_vector_type(8)));
typedef short s16x4 __attribute__((ext_vector_type(4)));
typedef __amdgpu_buffer_rsrc_t rsrc_t;
#define LAS __attribute__((address_space(3)))
#define DEV __device__ __forceinline__

constexpr int NN = 10000;
constexpr int NE = 160000;
constexpr int NR = 64;
constexpr int NW = 256;
constexpr int CHUNK = NE / NW;
constexpr int CAP = 32;
constexpr int LCAP = 3072;
constexpr int LDUM = LCAP + 64;
constexpr float NLOG2E = -1.4426950408889634f;
#define OUTAUX 16
#define SLEEP "2"
#define MIDS 1
constexpr int SC1 = 16;
#define GAUX 16

constexpr u32 OFF_EPOCH = 0;
constexpr u32 OFF_FLAGA = 4096;
constexpr u32 OFF_FLAGB = OFF_FLAGA + 8 * 4096;
constexpr u32 OFF_T = OFF_FLAGB + 8 * 4096;
constexpr u32 SZ_T = 4u * NN * 256u;
constexpr u32 OFF_CNT = OFF_T + SZ_T + 4096;
constexpr u32 OFF_IDX = OFF_CNT + NR * NW * 4;
constexpr u32 OFF_AUX = OFF_IDX + NR * NW * CAP * 4 + 4096;
constexpr u32 RSTRIDE = (NW * CAP + 32) * 32;
constexpr u32 NULLREC = NW * CAP;
constexpr u32 AUX_SZ = NR * RSTRIDE;
constexpr u32 OOB = 0x7FFFFF00u;

constexpr int L_XS = 0;
constexpr int L_WS = 81920;
constexpr int PT_C = 157;
constexpr int PT_S = 272;
constexpr int L_PT = 0;
constexpr int L_META = (PT_C + 16) * PT_S;
constexpr int L_EREC = L_META + LDUM * 4;
constexpr int L_SRC = L_EREC + 16 * 4096;
constexpr int L_REC = L_SRC + LDUM * 4;
static_assert(L_REC + 6400 <= 147456 && (L_EREC & 15) == 0 && (L_SRC & 15) == 0 && (L_REC & 15) == 0, "edge-phase overlay");
constexpr int L_CTR = 147456;
constexpr int L_H1 = L_CTR + 64;
constexpr int L_H2 = L_H1 + 512;
constexpr int L_NST = L_H2 + 1024;
constexpr int L_BIN = L_NST + 2048;
constexpr int L_LUT = L_BIN + 256;
constexpr int L_SCR = L_LUT + 384;
constexpr int L_TMP = L_SCR + 256;
constexpr int L_END = L_TMP + 6400;
static_assert(L_END <= 163840 && (L_TMP & 15) == 0, "LDS budget");

#define STAMP(i)
#define STAMPC(i)
#define STAMP_DECL
#define STAMP_FLUSH

DEV u32 fine_bin(u32 d) { return __umulhi(d * 64u, 6871948u); }
DEV u32 bin_start(u32 b) { return (b * 625u + 63u) >> 6; }
DEV u32 wswz(u32 k) { return ((k & 3) << 2) | ((k >> 2) & 3); }

DEV f32x4 mfma16(u32x4 a, u32x4 b, f32x4 c) {
    return __builtin_amdgcn_mfma_f32_16x16x32_bf16(__builtin_bit_cast(bf16x8, a), __builtin_bit_cast(bf16x8, b), c, 0, 0, 0);
}
DEV u32 cvtpk(float lo, float hi) {
    f32x2 v = {lo, hi};
    return __builtin_bit_cast(u32, __builtin_convertvector(v, bf16x2));
}
DEV float dot2(u32 w, u32 sel, float acc) {
    return __builtin_amdgcn_fdot2_f32_bf16(__builtin_bit_cast(bf16x2, w), __builtin_bit_cast(bf16x2, sel), acc, false);
}
DEV u32x2 trread(u32 lds_addr) {
    return __builtin_bit_cast(u32x2, __builtin_amdgcn_ds_read_tr16_b64_v4i16((LAS s16x4*)(uintptr_t)lds_addr));
}
DEV rsrc_t mkrsrc(const void* p, u32 bytes) { return __builtin_amdgcn_make_buffer_rsrc((void*)p, 0, bytes, 0x00020000); }
DEV u32x4 mksrd(const void* p, u32 bytes) {
    const u64 a = (u64)p;
    return u32x4{(u32)__builtin_amdgcn_readfirstlane((u32)a), (u32)__builtin_amdgcn_readfirstlane((u32)(a >> 32)),
                 (u32)__builtin_amdgcn_readfirstlane(bytes), 0x00020000u};
}
DEV u32 fresh_lane() {
    u32 l;
    asm volatile("v_mbcnt_lo_u32_b32 %0, -1, 0\n\tv_mbcnt_hi_u32_b32 %0, -1, %0" : "=v"(l));
    return l;
}
DEV u32 pack_hv(float hv, u32 lo) {
    const u32 m = __float_as_uint(fmaxf(__builtin_fabsf(hv), 7.8886091e-31f));
    const u32 t = m + 0x8000u - lo;
    return (t & 0x7fff0000u) | (__float_as_uint(hv) & 0x80000000u) | lo;
}
DEV u32x4 onehot8(int idx) {
    u32x4 r;
#pragma unroll
    for (int d = 0; d < 4; d++) r[d] = idx == 2 * d ? 0x00003F80u : (idx == 2 * d + 1 ? 0x3F800000u : 0u);
    return r;
}
DEV void st_agent(u32* p, u32 v) { __hip_atomic_store(p, v, __ATOMIC_RELAXED, __HIP_MEMORY_SCOPE_AGENT); }
DEV void barrier() {
    asm volatile("s_waitcnt lgkmcnt(0)\n\ts_barrier" ::: "memory");
}
DEV void wait_vm0() { __builtin_amdgcn_s_waitcnt(0x0F70); asm volatile("" ::: "memory"); }
DEV bool arrive_last(LAS char* lds, int ctr, u32 n, u32 lane) {
    const u32 a = (u32)(uintptr_t)lds + L_CTR + 4 * ctr;
    u32 v = 0, one = 1;
    asm volatile("s_waitcnt lgkmcnt(0)" ::: "memory");
    if (lane == 0) asm volatile("ds_add_rtn_u32 %0, %1, %2\n\ts_waitcnt lgkmcnt(0)" : "=&v"(v) : "v"(a), "v"(one) : "memory");
    return (u32)__builtin_amdgcn_readfirstlane(v) == n - 1;
}
DEV void wait_counter(LAS char* lds, int ctr, u32 n) {
    const u32 a = (u32)(uintptr_t)lds + L_CTR + 4 * ctr;
    u32 v;
    for (int it = 0; it < (1 << 22); it++) {
        asm volatile("ds_read_b32 %0, %1\n\ts_waitcnt lgkmcnt(0)" : "=&v"(v) : "v"(a) : "memory");
        if ((u32)__builtin_amdgcn_readfirstlane(v) >= n) break;
        asm volatile("s_sleep 1" ::: "memory");
    }
}
template <int CTRL, int RM> DEV u32 dpp0(u32 v) { return __builtin_amdgcn_update_dpp(0u, v, CTRL, RM, 0xf, true); }
DEV u32 scan16(u32 v) {
    v += dpp0<0x111, 0xf>(v); v += dpp0<0x112, 0xf>(v); v += dpp0<0x114, 0xf>(v); v += dpp0<0x118, 0xf>(v);
    return v;
}
DEV u32 scan64(u32 v) {
    v = scan16(v);
    v += __builtin_amdgcn_update_dpp(0u, v, 0x142, 0xa, 0xf, false);
    v += __builtin_amdgcn_update_dpp(0u, v, 0x143, 0xc, 0xf, false);
    return v;
}
DEV void group_barrier(LAS char* lds, int ctr, u32 n, u32 lane) {
    const u32 a = (u32)(uintptr_t)lds + L_CTR + 4 * ctr;
    u32 v, one = 1;
    asm volatile("s_waitcnt lgkmcnt(0)" ::: "memory");
    if (lane == 0) asm volatile("ds_add_u32 %0, %1" :: "v"(a), "v"(one) : "memory");
    for (int it = 0; it < (1 << 22); it++) {
        asm volatile("ds_read_b32 %0, %1\n\ts_waitcnt lgkmcnt(0)" : "=&v"(v) : "v"(a) : "memory");
        if (__builtin_amdgcn_readfirstlane(v) >= n) break;
        asm volatile("s_sleep 1" ::: "memory");
    }
}
DEV u32 mad16lo(u32 a, u32 b, u32 c) { u32 r; asm("v_mad_u32_u16 %0, %1, %2, %3" : "=v"(r) : "v"(a), "v"(b), "v"(c)); return r; }
DEV u32 mad16hi(u32 a, u32 b, u32 c) { u32 r; asm("v_mad_u32_u16 %0, %1, %2, %3 op_sel:[1,0,0,0]" : "=v"(r) : "v"(a), "v"(b), "v"(c)); return r; }
DEV void poll_flags(const char* ws, u32 off, u32 replica, u32 epoch, u32 lane) {
    const char* p = ws + off + replica * 4096 + lane * 16;
    for (int it = 0; it < (1 << 20); it++) {
        u32x4 v;
        asm volatile("global_load_dwordx4 %0, %1, off sc1\n\ts_waitcnt vmcnt(0)" : "=&v"(v) : "v"(p) : "memory");
        const bool ok = v.x == epoch && v.y == epoch && v.z == epoch && v.w == epoch;
        if (__all(ok)) break;
        asm volatile("s_sleep " SLEEP ::: "memory");
    }
}

#define RECPOL ""
#define GPOL 16
#define LV_MODE 0
#define LV_PRIO 1
struct Tile { u32x4 w[4]; u32x4 a; };
struct Next { u32x4 o; u32 m; };

struct EdgeLoop {
    rsrc_t rT;
    u32x4 rR;
    rsrc_t rRec;
    u32 ringw;
    const LAS u32* lsrc; const LAS uint16_t* lmeta;
    const LAS char* lrec;
    u32 c16, r256, abase, sel, half16, ring;
    u32x4 Bz[4], Bid;

    template <typename RecT>
    DEV u32 rec_off(int p0) const { return (u32)*(const LAS RecT*)(lrec + 4 * p0) * 32 + half16; }
    DEV void fetch(u32 off, int slot, u32 dep) const {
        const u32 m0v = ring + slot * 1024;
        asm volatile("s_mov_b32 m0, %2\n\ts_nop 0\n\tbuffer_load_dwordx4 %0, %1, 0 offen sc1 " RECPOL " lds" :: "v"(off), "s"(rR), "s"(m0v), "v"(dep) : "memory");
    }
    DEV Next lists(int pos) const { Next n; n.o = *(const LAS u32x4*)(lsrc + pos); n.m = lmeta[2 * pos]; return n; }
    DEV void row(Tile& T, const Next& n, int i) const { T.w[i] = __builtin_amdgcn_raw_buffer_load_b128(rT, mad16lo(n.o[i], r256, c16), 0, GPOL); }
    DEV void oper(Tile& T, const Next& n) const { T.a = *(const LAS u32x4*)(uintptr_t)((n.m << 4) + abase); }
    DEV void gathers(Tile& T, int pos) const {
        const u32x4 o = *(const LAS u32x4*)(lsrc + pos);
#pragma unroll
        for (int i = 0; i < 4; i++) T.w[i] = __builtin_amdgcn_raw_buffer_load_b128(rT, mad16lo(o[i], r256, c16), 0, GPOL);
    }
    DEV void operand(Tile& T, int pos) const { T.a = *(const LAS u32x4*)(uintptr_t)(((u32)lmeta[2 * pos] << 4) + abase); }
    DEV void issue(Tile& T, int pos) const { operand(T, pos); gathers(T, pos); }
    DEV void issue(Tile& T, const Next& n) const {
#pragma unroll
        for (int i = 0; i < 4; i++) row(T, n, i);
        oper(T, n);
    }
    template <bool ROLL>
    DEV void compute(Tile& T, u32x2 (&pk)[4], u32x2& S, const Next& n) const {
        f32x4 Z[4];
#pragma unroll
        for (int t = 0; t < 4; t++) Z[t] = mfma16(T.a, Bz[t], f32x4{0, 0, 0, 0});
        const f32x4 OH = mfma16(T.a, Bid, f32x4{0, 0, 0, 0});
        S.x = __builtin_amdgcn_perm(__float_as_uint(OH[1]), __float_as_uint(OH[0]), 0x07060302u);
        S.y = __builtin_amdgcn_perm(__float_as_uint(OH[3]), __float_as_uint(OH[2]), 0x07060302u);
        float msg[4][4];
#pragma unroll
        for (int i = 0; i < 4; i++) {
#pragma unroll
            for (int tp = 0; tp < 4; tp += 2) {
                const float z0 = dot2(T.w[i][tp], sel, Z[tp][i]), z1 = dot2(T.w[i][tp + 1], sel, Z[tp + 1][i]);
                f32x2 p = {__builtin_amdgcn_exp2f(z0), __builtin_amdgcn_exp2f(z1)};
                p = p + 1.0f;
                const f32x2 r = {__builtin_amdgcn_rcpf(p.x), __builtin_amdgcn_rcpf(p.y)};
                const f32x2 h = {__uint_as_float(T.w[i][tp]), __uint_as_float(T.w[i][tp + 1])};
                const f32x2 m = r * h;
                msg[i][tp] = m.x; msg[i][tp + 1] = m.y;
            }
            if (ROLL) {
                asm volatile("" : "+v"(msg[i][0]), "+v"(msg[i][1]), "+v"(msg[i][2]), "+v"(msg[i][3]));
                row(T, n, i);
                __builtin_amdgcn_sched_barrier(0);
            }
        }
        if (ROLL) oper(T, n);
#pragma unroll
        for (int t = 0; t < 4; t++) { pk[t].x = cvtpk(msg[0][t], msg[1][t]); pk[t].y = cvtpk(msg[2][t], msg[3][t]); }
    }
    DEV void compute(Tile& T, u32x2 (&pk)[4], u32x2& S) const { Next n; compute<false>(T, pk, S, n); }
    DEV void run_simple(f32x4 (&agg)[4], int pos, int nt) const {
        for (int t = 0; t < nt; t++) {
            if ((t & 1) == 0) fetch(rec_off<u32>(pos + 16 * t), (t >> 1) & 3, 0);
            Tile T0;
            u32x2 pk0[4], S0;
            gathers(T0, pos + 16 * t);
            asm volatile("s_waitcnt vmcnt(0)" ::: "memory");
            operand(T0, pos + 16 * t);
            compute(T0, pk0, S0);
#pragma unroll
            for (int k = 0; k < 4; k++)
                agg[k] = mfma16(u32x4{S0.x, S0.y, 0u, 0u}, u32x4{pk0[k].x, pk0[k].y, 0u, 0u}, agg[k]);
        }
    }
    DEV void prefetch(int pos, int nt) const {
        if (nt > 0) fetch(rec_off<uint16_t>(pos), 0, 0);
    }
    DEV void prio(int rem, int nt) const {
        if (3 * rem > 2 * nt) __builtin_amdgcn_s_setprio(2);
        else if (3 * rem > nt) __builtin_amdgcn_s_setprio(1);
        else __builtin_amdgcn_s_setprio(0);
    }
    DEV void agg2(f32x4 (&agg)[4], u32x2 S0, u32x2 S1, const u32x2 (&pk0)[4], const u32x2 (&pk1)[4]) const {
#pragma unroll
        for (int k = 0; k < 4; k++)
            agg[k] = mfma16(u32x4{S0.x, S0.y, S1.x, S1.y}, u32x4{pk0[k].x, pk0[k].y, pk1[k].x, pk1[k].y}, agg[k]);
    }
    DEV void agg1(f32x4 (&agg)[4], u32x2 S0, const u32x2 (&pk0)[4]) const {
#pragma unroll
        for (int k = 0; k < 4; k++)
            agg[k] = mfma16(u32x4{S0.x, S0.y, 0u, 0u}, u32x4{pk0[k].x, pk0[k].y, 0u, 0u}, agg[k]);
    }
    DEV void run(f32x4 (&agg)[4], Tile& T0, int pos, int nt) const {
        if (nt <= 0) return;
        Tile T1;
        u32x2 pk0[4], pk1[4], S0, S1;
        asm volatile("s_waitcnt vmcnt(0)" ::: "memory");
        if (nt > 2) fetch(rec_off<uint16_t>(pos + 32), 1, 0);
        operand(T0, pos);
        int t = 0, slot = 2;
        for (; t + 2 < nt; t += 2) {
            prio(nt - t, nt);
            const u32 roff = rec_off<uint16_t>(pos + 16 * (t + 4));
            issue(T1, pos + 16 * (t + 1));
            __builtin_amdgcn_sched_barrier(0);
            if (t + 4 < nt) fetch(roff, slot, T0.w[3].x);
            slot = (slot + 1) & 3;
            compute(T0, pk0, S0);
            __builtin_amdgcn_sched_barrier(0);
            issue(T0, pos + 16 * (t + 2));
            __builtin_amdgcn_sched_barrier(0);
            compute(T1, pk1, S1);
            agg2(agg, S0, S1, pk0, pk1);
            __builtin_amdgcn_sched_barrier(0);
        }
        if (t + 1 < nt) {
            issue(T1, pos + 16 * (t + 1));
            __builtin_amdgcn_sched_barrier(0);
            compute(T0, pk0, S0);
            __builtin_amdgcn_sched_barrier(0);
            compute(T1, pk1, S1);
            agg2(agg, S0, S1, pk0, pk1);
        } else {
            compute(T0, pk0, S0);
            agg1(agg, S0, pk0);
        }
    }
};

struct BinPlan { u32 start, tot, owner; int mybin; bool ok; };
template <bool SNAKE, bool WRITE>
DEV BinPlan plan_bins(LAS char* lds, u32 lane, int wv, u32 rstart, u32 r) {
    LAS u32* hist = (LAS u32*)(lds + L_H2);
    LAS u32x2* nst = (LAS u32x2*)(lds + L_NST);
    LAS u32* bin = (LAS u32*)(lds + L_BIN);
    const u32 j = lane & 15;
    const u32 n0 = bin_start(r * 16 + j) - rstart, nb = bin_start(r * 16 + j + 1) - rstart - n0;
    u32 h[10], tot = 0;
#pragma unroll
    for (int k = 0; k < 10; k++) h[k] = hist[n0 + k];
    const u32 ovf = bin[33];
    h[9] = nb > 9 ? h[9] : 0;
#pragma unroll
    for (int k = 0; k < 10; k++) tot += h[k];
    const u32 padded = (tot + 15) & ~15u;
    const u32 inc = scan16(padded);
    BinPlan p;
    p.start = inc - padded; p.tot = tot; p.owner = j; p.mybin = wv;
    p.ok = (u32)__builtin_amdgcn_readlane(inc, 15) <= (u32)LCAP && __builtin_amdgcn_readfirstlane(ovf) == 0;
    if (SNAKE) {
        u32 rk = 0;
#pragma unroll
        for (int jj = 0; jj < 16; jj++) { const u32 o = __builtin_amdgcn_readlane(padded, jj); rk += (o > padded || (o == padded && (u32)jj < j)) ? 1u : 0u; }
        const u32 s4 = rk >> 2;
        p.owner = 4 * s4 + ((s4 & 1) ? 3 - (rk & 3) : (rk & 3));
        p.mybin = __builtin_ctz((u32)__builtin_amdgcn_ballot_w64(p.owner == (u32)wv) & 0xffffu);
    }
    if (WRITE && p.ok && lane < 16) {
        const u32 lb = (u32)(uintptr_t)lds;
        const u32 w1 = (((lb + L_EREC) >> 4) + p.owner * 256) | (((lb + L_LUT) >> 4) << 16);
        u32 s = p.start << 16;
#pragma unroll
        for (int k = 0; k < 10; k++) { if (k < 9 || nb > 9) nst[n0 + k] = u32x2{s, w1 + (k << 16)}; s += h[k]; }
        bin[j] = p.start; bin[16 + j] = tot; bin[40 + p.owner] = j;
    }
    if (WRITE && lane == 0) bin[32] = p.ok;
    return p;
}
DEV void place(u32x2 nn, u32 rank, u32& pos, u32& meta) {
    const u32 rel = (nn.x & 0xffffu) + rank;
    pos = (nn.x >> 16) + rel;
    meta = nn.y + 2 * (rel & 127);
}
template <bool FAST>
DEV void pad_bin(LAS char* lds, u32 s, u32 l, u32 own, u32 nullrec, u32 lane) {
    const u32 q = s + l + lane;
    if (lane < 16 && l + lane < ((l + 15) & ~15u)) {
        ((LAS u32*)(lds + L_SRC))[q] = FAST ? nullrec << 16 : 0u;
        const u32 lb = (u32)(uintptr_t)lds;
        ((LAS u32*)(lds + L_META))[q] = ((((lb + L_EREC) >> 4) + own * 256) + 2 * ((l + lane) & 127)) | ((((lb + L_LUT) >> 4) + 15) << 16);
        if (!FAST) ((LAS u32*)(lds + L_REC))[q] = nullrec;
    }
}

__global__ void __launch_bounds__(1024, 4)
mpnn_fused(const uint16_t* __restrict__ x, const uint16_t* __restrict__ ea, const int* __restrict__ src, const int* __restrict__ dst,
           const uint16_t* __restrict__ uw, const uint16_t* __restrict__ vw, const uint16_t* __restrict__ gw,
           char* __restrict__ ws, uint16_t* __restrict__ out) {
    extern __shared__ __attribute__((aligned(16))) char smem[];
    LAS char* lds = (LAS char*)smem;
    const u32 lbase = (u32)(uintptr_t)lds;
    const int tid = threadIdx.x;
    const u32 lane = tid & 63;
    const int wv = __builtin_amdgcn_readfirstlane(tid >> 6);
    const u32 b = blockIdx.x;
    const u32 g = b & 3, r = ((b >> 3) << 1) + ((b >> 2) & 1);
    const u32 c = lane & 15, q = lane >> 4;
    const u32 rstart = bin_start(r * 16), rsize = bin_start(r * 16 + 16) - rstart;
    STAMP_DECL;
    STAMP(0);
    if (wv == 15) {
        u64 pc;
        asm volatile("s_getpc_b64 %0" : "=s"(pc));
        const char* c0 = (const char*)(pc & ~(u64)255);
#pragma unroll
        for (int i = 0; i < (CODEPF + 8191) / 8192; i++) {
            const char* a = c0 + min(64u * i + lane, (u32)(CODEPF / 128 - 1)) * 128;
            asm volatile("s_mov_b32 m0, %1\n\ts_nop 0\n\tglobal_load_lds_dword %0, off" :: "v"(a), "s"(lbase + L_SCR) : "memory");
        }
    }

    const bool g1 = wv >= 8;
    const int wg = wv & 7;
    const bool v1 = (u32)tid < (u32)(CHUNK - 512);
    if (tid < 16) ((LAS u32*)(lds + L_CTR))[tid] = 0;
    if (tid >= 64 && tid < 192) ((LAS u32*)(lds + L_H1))[tid - 64] = 0;
    if (tid >= 256 && tid < 512) ((LAS u32*)(lds + L_H2))[tid - 256] = 0;
    if (tid == 0) ((LAS u32*)(lds + L_BIN))[33] = 0;
    if (tid >= 512 && tid < 512 + 96) {
        const int i = tid - 512, row = i >> 2, d = i & 3;
        ((LAS u32*)(lds + L_LUT))[i] = row - 8 == 2 * d ? 0x00003F80u : (row - 8 == 2 * d + 1 ? 0x3F800000u : 0u);
    }
    asm volatile("" ::: "memory");

    const int mw = wg >> 2, tau = wv & 3;
    auto gemm = [&](f32x4 (&acc)[5][2], auto tr_c, auto mid) {
        constexpr bool TR = decltype(tr_c)::value;
        u32x4 B[2][2], A[2][5];
        u32 aW[2][2], aX[4];
#pragma unroll
        for (int h = 0; h < 2; h++) {
#pragma unroll
            for (int t = 0; t < 2; t++) {
                const u32 k = 8 * q + 4 * t + (c >> 2), ch = 2 * (tau + 4 * h) + ((lane & 3) >> 1);
                aW[h][t] = lbase + L_WS + 256 * k + 16 * (ch ^ wswz(k)) + 8 * (lane & 1);
                asm volatile("" : "+v"(aW[h][t]));
            }
        }
#pragma unroll
        for (int s4 = 0; s4 < 4; s4++) { aX[s4] = lbase + L_XS + (80 * mw + c) * 512 + 16 * ((4 * s4 + q) ^ c); asm volatile("" : "+v"(aX[s4])); }
        auto frags = [&](int s, u32x4 (&Bf)[2], u32x4 (&Af)[5]) {
#pragma unroll
            for (int h = 0; h < 2; h++) {
#pragma unroll
                for (int t = 0; t < 2; t++) {
                    const u32x2 v = trread(aW[h][t] + 8192 * s);
                    Bf[h][2 * t] = v.x; Bf[h][2 * t + 1] = v.y;
                }
            }
#pragma unroll
            for (int m = 0; m < 5; m++) Af[m] = *(const LAS u32x4*)(uintptr_t)(aX[s & 3] + 8192 * m + 256 * (s >> 2));
        };
#pragma unroll
        for (int m = 0; m < 5; m++) { acc[m][0] = f32x4{0, 0, 0, 0}; acc[m][1] = f32x4{0, 0, 0, 0}; }
        frags(0, B[0], A[0]);
#pragma unroll
        for (int s = 0; s < 8; s++) {
            if (s < 7) frags(s + 1, B[(s + 1) & 1], A[(s + 1) & 1]);
            __builtin_amdgcn_sched_barrier(0);
#pragma unroll
            for (int m = 0; m < 5; m++) {
                acc[m][0] = TR ? mfma16(B[s & 1][0], A[s & 1][m], acc[m][0]) : mfma16(A[s & 1][m], B[s & 1][0], acc[m][0]);
                acc[m][1] = TR ? mfma16(B[s & 1][1], A[s & 1][m], acc[m][1]) : mfma16(A[s & 1][m], B[s & 1][1], acc[m][1]);
            }
            __builtin_amdgcn_sched_barrier(0);
            if (s == MIDS) mid();
        }
    };
    auto stage_w = [&](const uint16_t* lo, const uint16_t* hi) {
        const u32 k = 4 * wg + (lane >> 4), ch = (lane & 15) ^ wswz(k);
        const char* p0 = (const char*)((ch < 8 ? lo + 8 * ch : hi + 8 * (ch - 8)) + k * 256 + 64 * g);
#pragma unroll
        for (int i = 0; i < 8; i++)
            __builtin_amdgcn_global_load_lds((const u32*)(p0 + i * 16384), (LAS u32*)(lds + L_WS + (wg + 8 * i) * 1024), 16, 0, 0);
    };

    u32 carry[50];
    u32 epoch;
    if (g1) {
        const u32 epoch_in = __hip_atomic_load((const u32*)(ws + OFF_EPOCH), __ATOMIC_RELAXED, __HIP_MEMORY_SCOPE_AGENT);
        barrier();
        {
            const u32 row = 2 * wg + (lane >> 5), ch = (lane & 31) ^ row;
            const rsrc_t rX = mkrsrc(x, NN * 512);
            const u32 o0 = (rstart + row) * 512 + ch * 16, omax = (NN - 1) * 512 + ch * 16;
#pragma unroll
            for (int i = 0; i < 10; i++)
                __builtin_amdgcn_raw_ptr_buffer_load_lds(rX, (LAS void*)(lds + L_XS + (wg + 8 * i) * 1024), 16, min(o0 + i * 8192, omax), 0, 0, 0);
        }
        wait_counter(lds, 4, 8);
        stage_w(gw + 256 * 256, vw);
        wait_vm0();
        epoch = __builtin_amdgcn_readfirstlane(epoch_in) + 1;
        STAMP(1);
        group_barrier(lds, 0, 8, lane);
        STAMP(2);
        f32x4 acc[5][2];
        gemm(acc, std::true_type{}, [] {});
        STAMP(3);
        (void)arrive_last(lds, 5, 8, lane);
        const rsrc_t rT = mkrsrc(ws + OFF_T + ((size_t)g * NN + rstart) * 256, rsize * 256);
        const u32 toff = (80 * mw + c) * 256 + (16 * tau + 4 * q) * 4;
#pragma unroll
        for (int m = 0; m < 5; m++) {
            u32x4 w;
#pragma unroll
            for (int i = 0; i < 4; i += 2) {
                const f32x2 xb = f32x2{acc[m][0][i], acc[m][0][i + 1]} * NLOG2E;
                const u32 pk = cvtpk(xb.x, xb.y);
                w[i] = pack_hv(acc[m][1][i], pk & 0xffffu);
                w[i + 1] = pack_hv(acc[m][1][i + 1], pk >> 16);
            }
            __builtin_amdgcn_raw_buffer_store_b128(w, rT, toff + m * 4096, 0, SC1);
        }
        STAMP(10);
        STAMP(10);
        wait_counter(lds, 6, 8);
        STAMP(4);
        f32x4 accP[5][2];
        gemm(accP, std::true_type{}, [&] {
            asm volatile("s_waitcnt vmcnt(0)" ::: "memory");
            if (arrive_last(lds, 7, 8, lane) && lane < REP) st_agent((u32*)(ws + OFF_FLAGB + lane * 4096) + b, epoch);
        });
#pragma unroll
        for (int m = 0; m < 5; m++) {
            carry[4 * m + 0] = cvtpk(accP[m][0][0] * NLOG2E, accP[m][0][1] * NLOG2E); carry[4 * m + 1] = cvtpk(accP[m][0][2] * NLOG2E, accP[m][0][3] * NLOG2E);
            carry[4 * m + 2] = cvtpk(accP[m][1][0], accP[m][1][1]); carry[4 * m + 3] = cvtpk(accP[m][1][2], accP[m][1][3]);
        }
#pragma unroll
        for (int i = 20; i < 50; i++) carry[i] = 0;
        STAMP(5);
    } else {
        LAS u32* h1 = (LAS u32*)(lds + L_H1);
        LAS u32* tslot = (LAS u32*)(lds + L_TMP);
        LAS u32* tword = tslot + 640;
        LAS char* exch = lds + L_WS;
        const u32 e0 = b * CHUNK + tid, e1 = v1 ? e0 + 512 : e0;
        u32 ed0 = dst[e0], ed1 = dst[e1], es0 = src[e0], es1 = src[e1];
        const u32x4 ea00 = *(const u32x4*)(ea + (size_t)e0 * 16), ea01 = *(const u32x4*)(ea + (size_t)e0 * 16 + 8);
        const u32x4 ea10 = *(const u32x4*)(ea + (size_t)e1 * 16), ea11 = *(const u32x4*)(ea + (size_t)e1 * 16 + 8);
        const u32 epoch_in = __hip_atomic_load((const u32*)(ws + OFF_EPOCH), __ATOMIC_RELAXED, __HIP_MEMORY_SCOPE_AGENT);
        barrier();
        __builtin_amdgcn_s_setprio(2);
        wait_vm0();
        epoch = __builtin_amdgcn_readfirstlane(epoch_in) + 1;
        asm volatile("" : "+v"(ed0), "+v"(ed1), "+v"(es0), "+v"(es1));
        STAMP(1);
        const u32 r0 = fine_bin(ed0) >> 4, r1 = fine_bin(ed1) >> 4;
        const u32 k0 = __hip_atomic_fetch_add(h1 + r0, 1u, __ATOMIC_RELAXED, __HIP_MEMORY_SCOPE_WORKGROUP);
        const u32 k1 = __hip_atomic_fetch_add(h1 + (v1 ? r1 : 64 + lane), 1u, __ATOMIC_RELAXED, __HIP_MEMORY_SCOPE_WORKGROUP);
        group_barrier(lds, 1, 8, lane);
        const u32 hc = h1[lane];
        const u32 st = scan64(hc) - hc;
        const u32 p0 = __shfl(st, r0, 64) + k0, p1 = __shfl(st, r1, 64) + k1;
        tslot[p0] = k0 < CAP ? (r0 * NW + b) * CAP + k0 : OOB;
        tword[p0] = (es0 << 8) | (ed0 - bin_start(r0 * 16));
        *(LAS u32x4*)(exch + p0 * 32) = ea00; *(LAS u32x4*)(exch + p0 * 32 + 16) = ea01;
        if (v1) {
            tslot[p1] = k1 < CAP ? (r1 * NW + b) * CAP + k1 : OOB;
            tword[p1] = (es1 << 8) | (ed1 - bin_start(r1 * 16));
            *(LAS u32x4*)(exch + p1 * 32) = ea10; *(LAS u32x4*)(exch + p1 * 32 + 16) = ea11;
        }
        group_barrier(lds, 2, 8, lane);
        STAMP(2);
        const rsrc_t rI = mkrsrc(ws + OFF_IDX, NR * NW * CAP * 4);
        const rsrc_t rp = mkrsrc(ws + OFF_AUX, AUX_SZ);
        u32x4 ev[3]; u32 eo[3], io[2], iw[2];
#pragma unroll
        for (int i = 0; i < 3; i++) {
            const u32 jj = min(tid + 512u * i, 2u * CHUNK - 1);
            const u32 sl = tslot[jj >> 1];
            ev[i] = *(const LAS u32x4*)(exch + jj * 16);
            eo[i] = (sl == OOB || tid + 512u * i >= 2u * CHUNK) ? OOB : sl * 32 + (sl >> 13) * 1024 + (jj & 1) * 16;
        }
#pragma unroll
        for (int i = 0; i < 2; i++) {
            const u32 ps = min(tid + 512u * i, (u32)CHUNK - 1);
            const u32 sl = tslot[ps];
            iw[i] = tword[ps];
            io[i] = (sl == OOB || tid + 512u * i >= (u32)CHUNK) ? OOB : sl * 4;
        }
        const u32 hcnt = h1[tid & 63];
        asm volatile("" : "+v"(ev[0]), "+v"(ev[1]), "+v"(ev[2]), "+v"(iw[0]), "+v"(iw[1]));
        (void)arrive_last(lds, 4, 8, lane);
#pragma unroll
        for (int i = 0; i < 2; i++) __builtin_amdgcn_raw_buffer_store_b32(iw[i], rI, io[i], 0, SC1);
#pragma unroll
        for (int i = 0; i < 3; i++) __builtin_amdgcn_raw_buffer_store_b128(ev[i], rp, eo[i], 0, SC1);
        if (tid < NR) st_agent((u32*)(ws + OFF_CNT) + tid * NW + b, hcnt);
        if (b < NR && tid >= 64 && tid < 72) st_agent((u32*)(ws + OFF_AUX + b * RSTRIDE + NULLREC * 32) + (tid - 64), 0u);
        STAMP(3);
        asm volatile("s_waitcnt vmcnt(0)" ::: "memory");
        if (arrive_last(lds, 3, 8, lane) && lane < REP) st_agent((u32*)(ws + OFF_FLAGA + lane * 4096) + b, epoch);
        STAMP(4);
        wait_counter(lds, 0, 8);
        u32x4 wp[8];
        {
            const u32 k = 4 * wg + (lane >> 4), ch = lane & 15;
            const char* p0 = (const char*)((ch < 8 ? gw + 8 * ch : uw + 8 * (ch - 8)) + k * 256 + 64 * g);
#pragma unroll
            for (int i = 0; i < 8; i++) wp[i] = *(const u32x4*)(p0 + i * 16384);
        }
        asm volatile("" ::: "memory");
        if (wv == 0) poll_flags(ws, OFF_FLAGA, b & (REP - 1), epoch, lane);
        group_barrier(lds, 8, 8, lane);
        STAMP(5);
        const u32 cell = r * NW + (tid >> 1), j2 = tid & 1;
        const rsrc_t rC = mkrsrc(ws + OFF_CNT, NR * NW * 4 + NR * NW * CAP * 4);
        carry[48] = gw[(512 + 2 * wg) * 256 + 64 * g + lane]; carry[49] = gw[(513 + 2 * wg) * 256 + 64 * g + lane];
        const u32 cnt = __builtin_amdgcn_raw_buffer_load_b32(rC, cell * 4, 0, SC1);
        u32x4 iv[4];
#pragma unroll
        for (int i = 0; i < 4; i++) iv[i] = __builtin_amdgcn_raw_buffer_load_b128(rC, NR * NW * 4 + (cell * CAP + 4 * (j2 + 2 * i)) * 4, 0, SC1);
        asm volatile("" ::: "memory");
        asm volatile("" ::: "memory");
        {
            u32 pw[8][4];
#pragma unroll
            for (int i = 0; i < 8; i++) {
#pragma unroll
                for (int t = 0; t < 4; t++)
                    pw[i][t] = __builtin_amdgcn_perm(wp[i][2 + (t >> 1)], wp[i][t >> 1], (t & 1) ? 0x07060302u : 0x05040100u);
                asm volatile("" : "+v"(pw[i][0]), "+v"(pw[i][1]), "+v"(pw[i][2]), "+v"(pw[i][3]));
            }
            const u32 k = 4 * wg + (lane >> 4), ch = lane & 15, sw = wswz(k);
            u32 a[4];
#pragma unroll
            for (int t = 0; t < 4; t++) { a[t] = lbase + L_WS + k * 256 + 4 * (ch & 3) + 16 * ((8 * (ch >> 3) + 2 * t + ((ch >> 2) & 1)) ^ sw); asm volatile("" : "+v"(a[t])); }
            wait_counter(lds, 5, 8);
#pragma unroll
            for (int i = 0; i < 8; i++) {
#pragma unroll
                for (int t = 0; t < 4; t++) *(LAS u32*)(uintptr_t)(a[t] + i * 8192) = pw[i][t];
            }
        }
        (void)arrive_last(lds, 6, 8, lane);
        STAMP(9);
        if (cnt > (u32)CAP) ((LAS u32*)(lds + L_BIN))[33] = 1;
        const u32 cn = min(cnt, (u32)CAP);
        const bool more = __any(cn > 16);
        LAS u32* hist = (LAS u32*)(lds + L_H2);
        u32 key[16], rank[16];
        const u32 dummy = 192u + lane;
#pragma unroll
        for (int k = 0; k < 8; k++) {
            key[k] = 4 * (j2 + 2 * (k >> 2)) + (k & 3) < cn ? (iv[k >> 2][k & 3] & 0xff) : dummy;
            rank[k] = __hip_atomic_fetch_add(&hist[key[k]], 1u, __ATOMIC_RELAXED, __HIP_MEMORY_SCOPE_WORKGROUP);
        }
        if (more) {
#pragma unroll
            for (int k = 8; k < 16; k++) {
                key[k] = 4 * (j2 + 2 * (k >> 2)) + (k & 3) < cn ? (iv[k >> 2][k & 3] & 0xff) : dummy;
                rank[k] = __hip_atomic_fetch_add(&hist[key[k]], 1u, __ATOMIC_RELAXED, __HIP_MEMORY_SCOPE_WORKGROUP);
            }
        } else {
#pragma unroll
            for (int k = 8; k < 16; k++) { key[k] = dummy; rank[k] = 0; }
        }
        group_barrier(lds, 9, 8, lane);
        STAMP(6);
        const BinPlan bp = plan_bins<true, true>(lds, lane, wv, rstart, r);
        const LAS u32x2* nst = (const LAS u32x2*)(lds + L_NST);
        const u32 recb = ((tid >> 1) * CAP + 4 * j2) << 16;
        auto entry = [&](int k, u32x2 nn) {
            u32 ps, mt;
            place(nn, rank[k], ps, mt);
            carry[16 + k] = key[k] < 192u ? ps : LCAP + lane;
            carry[32 + k] = mt;
            carry[k] = (iv[k >> 2][k & 3] >> 8) | (recb + ((8 * (k >> 2) + (k & 3)) << 16));
        };
        u32x2 nn[16];
#pragma unroll
        for (int k = 0; k < 8; k++) nn[k] = nst[key[k]];
#pragma unroll
        for (int k = 0; k < 8; k++) entry(k, nn[k]);
        if (more) {
#pragma unroll
            for (int k = 8; k < 16; k++) nn[k] = nst[key[k]];
#pragma unroll
            for (int k = 8; k < 16; k++) entry(k, nn[k]);
        } else {
#pragma unroll
            for (int k = 8; k < 16; k++) { carry[k] = 0; carry[16 + k] = 0xFFFFFFFFu; carry[32 + k] = 0; }
        }
        (void)bp;
        STAMP(10);
    }
    barrier();
    STAMP(7);

    const u32 ln2 = fresh_lane();
    LAS u32* bin = (LAS u32*)(lds + L_BIN);
    const bool fast = __builtin_amdgcn_readfirstlane(bin[32]) != 0;
    const int mybin = fast ? __builtin_amdgcn_readfirstlane(bin[40 + wv]) : wv;
    const int lpos = __builtin_amdgcn_readfirstlane(bin[mybin & 15]), llen = __builtin_amdgcn_readfirstlane(bin[16 + (mybin & 15)]);
    if (g1) {
#pragma unroll
        for (int m = 0; m < 5; m++) {
            const u32 row = 80 * mw + 16 * m + c;
            if (row < (u32)PT_C) {
                *(LAS u32x2*)(lds + L_PT + row * PT_S + (16 * tau + 4 * q) * 2) = u32x2{carry[4 * m], carry[4 * m + 1]};
                *(LAS u32x2*)(lds + L_PT + row * PT_S + 128 + (16 * tau + 4 * q) * 2) = u32x2{carry[4 * m + 2], carry[4 * m + 3]};
            }
        }
    } else {
        const u32 cwp = cvtpk(__uint_as_float(carry[48] << 16) * NLOG2E, __uint_as_float(carry[49] << 16) * NLOG2E);
        LAS uint16_t* crow = (LAS uint16_t*)(lds + L_PT + (PT_C + 2 * wg) * PT_S) + 16 * (ln2 & 3) + (ln2 >> 2);
        crow[0] = (uint16_t)cwp; crow[PT_S / 2] = (uint16_t)(cwp >> 16);
    }
    if (!g1 && fast) {
        LAS u32* lsrc = (LAS u32*)(lds + L_SRC);
        LAS u32* lmeta = (LAS u32*)(lds + L_META);
#pragma unroll
        for (int k = 0; k < 8; k++) { lsrc[carry[16 + k]] = carry[k]; lmeta[carry[16 + k]] = carry[32 + k]; }
        if (__any(carry[24] != 0xFFFFFFFFu)) {
#pragma unroll
            for (int k = 8; k < 16; k++) { lsrc[carry[16 + k]] = carry[k]; lmeta[carry[16 + k]] = carry[32 + k]; }
        }
    }
    if (fast) pad_bin<true>(lds, lpos, llen, wv, NULLREC, ln2);
    barrier();
    __builtin_amdgcn_s_setprio(3);
    STAMP(8);

    const int obin = fast ? mybin : wv;
    const u32 nstart = bin_start(r * 16 + obin), bsize = bin_start(r * 16 + obin + 1) - nstart;
    f32x4 agg[4];
    auto setup = [&](EdgeLoop& L, bool cells) {
        const u32 ln = fresh_lane(), cs = ln & 15, qs = ln >> 4;
        L.rT = mkrsrc(ws + OFF_T + (size_t)g * NN * 256, NN * 256);
        L.rR = mksrd(cells ? (const void*)(ws + OFF_AUX + r * RSTRIDE) : (const void*)ea, cells ? RSTRIDE : NE * 32);
        L.lsrc = (const LAS u32*)(lds + L_SRC) + 4 * qs; L.lmeta = (const LAS uint16_t*)(lds + L_META) + 2 * cs + (qs < 2 ? 0 : 1);
        L.lrec = lds + (cells ? L_SRC + 2 : L_REC) + (ln >> 1) * 4;
        L.c16 = cs * 16; L.half16 = (ln & 1) * 16;
        L.ring = lbase + L_EREC + wv * 4096;
        L.ringw = L.ring + ln * 16;
        L.rRec = mkrsrc(cells ? (const void*)(ws + OFF_AUX + r * RSTRIDE) : (const void*)ea, cells ? RSTRIDE : NE * 32);
        L.sel = 0x00003F80u;
        L.r256 = 256;
        asm volatile("" : "+v"(L.sel), "+v"(L.r256));
        L.abase = qs < 2 ? 16 * qs : 16 * (8 - 8 * (qs - 2));
    };
    auto operands = [&](EdgeLoop& L) {
        const u32 ln = fresh_lane(), cs = ln & 15, qs = ln >> 4;
        const u32 rr = 8 * (qs & 1) + (cs >> 2), row0 = nstart - rstart;
        const u32 col = 8 * (ln & 3);
        const u32 pz0 = lbase + L_PT + (qs < 2 ? PT_C + rr : row0 + rr) * PT_S + col;
        const u32 pz1 = pz0 + 4 * PT_S;
        const u32 pu0 = lbase + L_PT + min(row0 + rr, rsize - 1) * PT_S + 128 + col;
        const u32 pu1 = lbase + L_PT + min(row0 + rr + 4, rsize - 1) * PT_S + 128 + col;
        const LAS u32x4* lut = (const LAS u32x4*)(lds + L_LUT);
        const u32x4 aid = lut[qs < 2 ? cs + 8 - 8 * qs : 0];
        L.Bid = lut[qs < 2 ? 0 : cs + 24 - 8 * qs];
#pragma unroll
        for (int t = 0; t < 4; t++) {
            const u32x2 z0 = trread(pz0 + 32 * t), z1 = trread(pz1 + 32 * t), u0 = trread(pu0 + 32 * t), u1 = trread(pu1 + 32 * t);
            L.Bz[t] = u32x4{z0.x, z0.y, z1.x, z1.y};
            agg[t] = mfma16(aid, u32x4{u0.x, u0.y, u1.x, u1.y}, f32x4{0, 0, 0, 0});
        }
    };

    if (fast) {
        EdgeLoop L;
        setup(L, true);
        const int lnt = (llen + 15) >> 4;
        Tile T0;
        if (wv == 8) { poll_flags(ws, OFF_FLAGB, b & (REP - 1), epoch, lane); (void)arrive_last(lds, 11, 1, lane); }
        L.prefetch(lpos, lnt);
        operands(L);
        wait_counter(lds, 11, 1);
        L.gathers(T0, lpos);
        STAMP(11);
        STAMPC(14);
        L.run(agg, T0, lpos, lnt);
    } else {
        EdgeLoop L;
        setup(L, false);
        operands(L);
        if (wv == 8) poll_flags(ws, OFF_FLAGB, b & (REP - 1), epoch, lane);
        const u32 ln = fresh_lane();
        const int td = wv * 64 + ln;
        LAS u32* hist = (LAS u32*)(lds + L_H2);
        for (int e0 = 0; e0 < NE; e0 += 1024) {
            barrier();
            if (td < 256) hist[td] = 0;
            if (td == 0) bin[33] = 0;
            barrier();
            u32 wd = 0, key = 192u + ln;
            const int ee = e0 + td;
            if (ee < NE) {
                const u32 d = dst[ee];
                if ((fine_bin(d) >> 4) == r) { wd = src[ee]; key = d - rstart; }
            }
            const u32 rank = __hip_atomic_fetch_add(&hist[key], 1u, __ATOMIC_RELAXED, __HIP_MEMORY_SCOPE_WORKGROUP);
            barrier();
            const BinPlan sp = plan_bins<false, true>(lds, ln, wv, rstart, r);
            const u32x2 nn = ((const LAS u32x2*)(lds + L_NST))[key];
            if (key < 192u) {
                u32 ps, mt;
                place(nn, rank, ps, mt);
                ((LAS u32*)(lds + L_SRC))[ps] = wd;
                ((LAS u32*)(lds + L_META))[ps] = mt;
                ((LAS u32*)(lds + L_REC))[ps] = ee;
            }
            const int pos = __builtin_amdgcn_readlane(sp.start, wv), len = __builtin_amdgcn_readlane(sp.tot, wv);
            pad_bin<false>(lds, pos, len, wv, 0u, ln);
            barrier();
            L.run_simple(agg, pos, (len + 15) >> 4);
        }
    }
    if (b == 0 && wv == 8 && (threadIdx.x & 63) == 0) st_agent((u32*)(ws + OFF_EPOCH), epoch);
    STAMP(12);
    STAMPC(15);

    const u32 le = fresh_lane(), ce = le & 15, qe = le >> 4;
    const rsrc_t rO = mkrsrc(out + (size_t)nstart * 256, bsize * 512);
#pragma unroll
    for (int i = 0; i < 4; i++) {
        float y[4];
#pragma unroll
        for (int t = 0; t < 4; t++) {
            const float v = agg[t][i];
            y[t] = v * __builtin_amdgcn_rcpf(1.0f + __builtin_amdgcn_exp2f(v * NLOG2E));
        }
        __builtin_amdgcn_raw_buffer_store_b64(u32x2{cvtpk(y[0], y[1]), cvtpk(y[2], y[3])}, rO, (4 * qe + i) * 512 + 128 * g + 8 * ce, 0, OUTAUX);
    }
    STAMP(13);
    STAMP_FLUSH;
}

extern "C" void kernel_launch(void* const* d_in, const int* in_sizes, int n_in,
                              void* d_out, int out_size, void* d_ws, size_t ws_size,
                              hipStream_t stream) {
    const uint16_t* x = (const uint16_t*)d_in[0];
    const uint16_t* ea = (const uint16_t*)d_in[1];
    const int* src = (const int*)d_in[2];
    const int* dst = (const int*)d_in[3];
    const uint16_t* uw = (const uint16_t*)d_in[4];
    const uint16_t* vw = (const uint16_t*)d_in[5];
    const uint16_t* gw = (const uint16_t*)d_in[6];
    (void)hipFuncSetAttribute((const void*)mpnn_fused, hipFuncAttributeMaxDynamicSharedMemorySize, L_END);
    mpnn_fused<<<256, 1024, L_END, stream>>>(x, ea, src, dst, uw, vw, gw, (char*)d_ws, (uint16_t*)d_out);
}
```
